```python
import torch

def _softmax(x, axis=-1):
    return torch.softmax(x, dim=axis)
def _gelu(x):
    return torch.nn.functional.gelu(x, approximate="tanh")
def _silu(x):
    return torch.nn.functional.silu(x)
def _sigmoid(x):
    return torch.sigmoid(x)
def _layernorm(x, axis=-1, eps=1e-5):
    m = x.mean(dim=axis, keepdim=True)
    v = x.var(dim=axis, keepdim=True, unbiased=False)
    return (x - m) / torch.sqrt(v + eps)
def _scatter_add(tgt, idx, src, axis=0):
    return tgt.clone().index_add_(axis, idx, src)
def _segment_sum(src, seg, n):
    out = torch.zeros((n,) + src.shape[1:], dtype=src.dtype)
    return out.index_add_(0, seg, src)
def _pad(x, pw):
    flat = tuple(v for pair in reversed(pw) for v in pair)
    return torch.nn.functional.pad(x, flat)

kernel_name = 'parallel_sum__gqa_block_cached__glu_gated_geglu__decode'

def setup_inputs():

    _w = (lambda a: (lambda t: t.to(torch.bfloat16) if t.is_floating_point() else t)(torch.as_tensor(a)))
    def _A_setup():
        B, Sq, D = 8, 1, 1024
        Hq, Hkv, Sk, Dh = 32, 4, 8192, 128
        assert Hq % Hkv == 0, (Hq, Hkv)
        return {
            "x": _w((lambda *s: torch.randn(*s, dtype=torch.bfloat16) * 0.05)(B, Sq, D)),
            "k_cache": _w((lambda *s: torch.randn(*s, dtype=torch.bfloat16) * 1.6)(B, Hkv, Sk, Dh)),
            "v_cache": _w((lambda *s: torch.randn(*s, dtype=torch.bfloat16) * 0.8)(B, Hkv, Sk, Dh)),
            "wq": _w((lambda *s: torch.randn(*s, dtype=torch.bfloat16) * 1.6)(D, Hq * Dh) * (D ** -0.5)),
            "wo": _w((lambda *s: torch.randn(*s, dtype=torch.bfloat16) * 0.8)(Hq * Dh, D) * ((Hq * Dh) ** -0.5)),
        }
    _A_in = _A_setup()

    def _B_setup():
        B, S, Dm, Df = 8, 1, 1024, 3584
        return {
            "x": _w((lambda *s: torch.randn(*s, dtype=torch.bfloat16) * 0.05)(B, S, Dm)),
            "w_gate": _w((lambda *s: torch.randn(*s, dtype=torch.bfloat16) * 0.4)(Dm, Df) * (Dm ** -0.5)),
            "w_up": _w((lambda *s: torch.randn(*s, dtype=torch.bfloat16) * 0.4)(Dm, Df) * (Dm ** -0.5)),
            "w_down": _w((lambda *s: torch.randn(*s, dtype=torch.bfloat16) * 0.4)(Df, Dm) * (Df ** -0.5)),
        }
    _B_in = _B_setup()

    return {
        "A_x": _A_in["x"],
        "A_k_cache": _A_in["k_cache"],
        "A_v_cache": _A_in["v_cache"],
        "A_wq": _A_in["wq"],
        "A_wo": _A_in["wo"],
        "B_x": _B_in["x"],
        "B_w_gate": _B_in["w_gate"],
        "B_w_up": _B_in["w_up"],
        "B_w_down": _B_in["w_down"],
    }

def reference(A_x, A_k_cache, A_v_cache, A_wq, A_wo, B_x, B_w_gate, B_w_up, B_w_down):
    def _A_ref(x, k_cache, v_cache, wq, wo):
        B, Sq, D = x.shape
        Hq, Dh = 32, 128
        Hkv = k_cache.shape[1]
        rep = Hq // Hkv
        q = (lambda x, *d: x.permute(*d))((x @ wq).reshape(B, Sq, Hq, Dh), 0, 2, 1, 3)
        k = (lambda x, n, axis: torch.repeat_interleave(x, n, dim=axis))(k_cache, rep, axis=1)
        v = (lambda x, n, axis: torch.repeat_interleave(x, n, dim=axis))(v_cache, rep, axis=1)
        scores = torch.einsum("bhqd,bhkd->bhqk", q, k) * (Dh ** -0.5)
        p = _softmax(scores, axis=-1)
        o = torch.einsum("bhqk,bhkd->bhqd", p, v)
        return (lambda x, *d: x.permute(*d))(o, 0, 2, 1, 3).reshape(B, Sq, Hq * Dh) @ wo

    def _B_ref(x, w_gate, w_up, w_down):
        g = torch.einsum("bsd,df->bsf", x, w_gate)
        u = torch.einsum("bsd,df->bsf", x, w_up)
        h = _gelu(g) * u
        return torch.einsum("bsf,fd->bsd", h, w_down)

    x = A_x
    xn = _layernorm(x, axis=-1)
    return x + _A_ref(xn, A_k_cache, A_v_cache, A_wq, A_wo) + _B_ref(xn, B_w_gate, B_w_up, B_w_down)
```

```cpp
#include <hip/hip_runtime.h>
#include <hip/hip_bf16.h>
#include <stdint.h>

namespace {

typedef unsigned v4u __attribute__((ext_vector_type(4)));
typedef unsigned v2u __attribute__((ext_vector_type(2)));
typedef float f32x4 __attribute__((ext_vector_type(4)));
typedef __bf16 bf16x8 __attribute__((ext_vector_type(8)));
typedef __bf16 bf16x2 __attribute__((ext_vector_type(2)));
typedef short s16x4 __attribute__((ext_vector_type(4)));
#define LAS __attribute__((address_space(3)))

constexpr int NWG = 256, NTHR = 768;
constexpr int DM = 1024, DFF = 3584;
constexpr int AUX_NT = 2, AUX_SC1 = 16;
constexpr int RSRC_W3 = 0x00020000;

constexpr int WS_EPOCH = 0;
constexpr int WS_QP = 4096;
constexpr int WS_QF = WS_QP + 32 * 8 * 8 * 1024;
constexpr int WS_GP = WS_QF + 8 * 32 * 512;
constexpr int WS_HF = WS_GP + 28 * 8 * 8 * 512;
constexpr int WS_OP = WS_HF + 8 * 896 * 16;
constexpr int WS_ML = WS_OP + 32 * 8 * 8 * 512;
constexpr int WS_FP = WS_ML + 32 * 8 * 8 * 128;
constexpr int WS_END = WS_FP + 8 * 32 * 32 * 256;

constexpr int L_K = 0;
constexpr int L_X = 131072;
constexpr int XPITCH = 2064;
constexpr int L_Q = L_X + 8 * XPITCH;
constexpr int QPITCH = 272;
constexpr int L_A2 = L_Q + 8 * QPITCH;
constexpr int APITCH = 528;
constexpr int L_MM = L_A2 + 8 * APITCH;
constexpr int L_FLG = L_MM + 8 * 16 * 8;
constexpr int F_XN = 0, F_RED1 = 1, F_RED2 = 2, F_FREE1 = 3, F_FREE2 = 4, F_XRAW = 5;
constexpr int L_END = L_FLG + 32;
constexpr int LDS_BYTES = 163840;
static_assert(L_END <= LDS_BYTES, "lds");

constexpr int G1PRE = 8, G1GAP = 8, H3PRE = 3, H4PRE = 4;
constexpr unsigned SPIN_CAP = 1u << 15;

struct Params {
  const void* x;
  const void* k;
  const void* v;
  const void* wq;
  const void* wo;
  const void* wg;
  const void* wu;
  const void* wd;
  void* out;
  void* ws;
};

typedef __amdgpu_buffer_rsrc_t rsrc_t;
__device__ __forceinline__ rsrc_t mk_rsrc(const void* p, int bytes) {
  return __builtin_amdgcn_make_buffer_rsrc(const_cast<void*>(p), 0, bytes, RSRC_W3);
}
__device__ __forceinline__ v4u ld_nt(rsrc_t r, int voff, int soff) { return __builtin_amdgcn_raw_buffer_load_b128(r, voff, soff, AUX_NT); }
__device__ __forceinline__ v4u ld_sc1(rsrc_t r, int voff) { return __builtin_amdgcn_raw_buffer_load_b128(r, voff, 0, AUX_SC1); }
__device__ __forceinline__ void st_sc1(rsrc_t r, int voff, v4u v) { __builtin_amdgcn_raw_buffer_store_b128(v, r, voff, 0, AUX_SC1); }
__device__ __forceinline__ void st_sc1_b64(rsrc_t r, int voff, v2u v) { __builtin_amdgcn_raw_buffer_store_b64(v, r, voff, 0, AUX_SC1); }
#define SB() __builtin_amdgcn_sched_barrier(0)
#define BARRIER() asm volatile("s_waitcnt lgkmcnt(0)\n\ts_barrier" ::: "memory")
#define PACE(n) asm volatile("s_waitcnt vmcnt(" #n ")" ::: "memory")

__device__ __forceinline__ float bf_lo(unsigned u) { return __builtin_bit_cast(float, u << 16); }
__device__ __forceinline__ float bf_hi(unsigned u) { return __builtin_bit_cast(float, u & 0xffff0000u); }
__device__ __forceinline__ unsigned pack2(float lo, float hi) {
  bf16x2 v = {(__bf16)lo, (__bf16)hi};
  return __builtin_bit_cast(unsigned, v);
}
__device__ __forceinline__ float bfr(float x) { return bf_lo(pack2(x, 0.f)); }
template <int CTRL> __device__ __forceinline__ float dppf(float x) {
  return __builtin_bit_cast(float, __builtin_amdgcn_mov_dpp(__builtin_bit_cast(int, x), CTRL, 0xf, 0xf, true));
}
constexpr int XOR1 = 0xB1, XOR2 = 0x4E, ROR4 = 0x124, ROR8 = 0x128, HMIR = 0x141;
__device__ __forceinline__ float xrow_sum(float x) {
  auto s = __builtin_amdgcn_permlane16_swap(__float_as_uint(x), __float_as_uint(x), false, false);
  x = __uint_as_float(s[0]) + __uint_as_float(s[1]);
  auto t = __builtin_amdgcn_permlane32_swap(__float_as_uint(x), __float_as_uint(x), false, false);
  return __uint_as_float(t[0]) + __uint_as_float(t[1]);
}
__device__ __forceinline__ float xrow_max(float x) {
  auto s = __builtin_amdgcn_permlane16_swap(__float_as_uint(x), __float_as_uint(x), false, false);
  x = fmaxf(__uint_as_float(s[0]), __uint_as_float(s[1]));
  auto t = __builtin_amdgcn_permlane32_swap(__float_as_uint(x), __float_as_uint(x), false, false);
  return fmaxf(__uint_as_float(t[0]), __uint_as_float(t[1]));
}
__device__ __forceinline__ float half_sum(float x) {
  auto t = __builtin_amdgcn_permlane32_swap(__float_as_uint(x), __float_as_uint(x), false, false);
  return __uint_as_float(t[0]) + __uint_as_float(t[1]);
}
__device__ __forceinline__ float wave_sum(float x) {
  x += dppf<ROR8>(x);
  x += dppf<ROR4>(x);
  x += dppf<XOR2>(x);
  x += dppf<XOR1>(x);
  return xrow_sum(x);
}
__device__ __forceinline__ f32x4 mfma32(v4u a, v4u b, f32x4 c) {
  return __builtin_amdgcn_mfma_f32_16x16x32_bf16(__builtin_bit_cast(bf16x8, a), __builtin_bit_cast(bf16x8, b), c, 0, 0, 0);
}
__device__ __forceinline__ f32x4 mfma16(v2u a, v2u b, f32x4 c) {
  return __builtin_amdgcn_mfma_f32_16x16x16bf16_1k(__builtin_bit_cast(s16x4, a), __builtin_bit_cast(s16x4, b), c, 0, 0, 0);
}
template <int J> __device__ __forceinline__ unsigned tr2(const v4u& lo, const v4u& hi) {
  return __builtin_amdgcn_perm(hi[J >> 1], lo[J >> 1], (J & 1) ? 0x07060302u : 0x05040100u);
}
template <int J> __device__ __forceinline__ void tile32_step(const v4u (&r)[8], v4u a, f32x4 (&acc)[8]) {
  v4u b = {tr2<J>(r[0], r[1]), tr2<J>(r[2], r[3]), tr2<J>(r[4], r[5]), tr2<J>(r[6], r[7])};
  acc[J] = mfma32(a, b, acc[J]);
}
__device__ __forceinline__ void tile32(const v4u (&r)[8], v4u a, f32x4 (&acc)[8]) {
  tile32_step<0>(r, a, acc); tile32_step<1>(r, a, acc); tile32_step<2>(r, a, acc); tile32_step<3>(r, a, acc);
  tile32_step<4>(r, a, acc); tile32_step<5>(r, a, acc); tile32_step<6>(r, a, acc); tile32_step<7>(r, a, acc);
}
template <int J> __device__ __forceinline__ void tile16_step(const v4u (&r)[4], v2u a, f32x4 (&acc)[8]) {
  v2u b = {tr2<J>(r[0], r[1]), tr2<J>(r[2], r[3])};
  acc[J] = mfma16(a, b, acc[J]);
}
__device__ __forceinline__ void tile16(const v4u (&r)[4], v2u a, f32x4 (&acc)[8]) {
  tile16_step<0>(r, a, acc); tile16_step<1>(r, a, acc); tile16_step<2>(r, a, acc); tile16_step<3>(r, a, acc);
  tile16_step<4>(r, a, acc); tile16_step<5>(r, a, acc); tile16_step<6>(r, a, acc); tile16_step<7>(r, a, acc);
}
__device__ __forceinline__ void zero8(f32x4 (&acc)[8]) {
#pragma unroll
  for (int j = 0; j < 8; ++j) acc[j] = f32x4{0.f, 0.f, 0.f, 0.f};
}
__device__ __forceinline__ void store_tile(LAS unsigned char* dst, const f32x4 (&acc)[8], int g, int r) {
  const bool hi = g >= 2;
  LAS unsigned char* base = dst + (4 * (g & 1)) * 512 + (g >> 1) * 256 + r * 16;
#pragma unroll
  for (int reg = 0; reg < 4; ++reg) {
    const f32x4 v = {hi ? acc[4][reg] : acc[0][reg], hi ? acc[5][reg] : acc[1][reg], hi ? acc[6][reg] : acc[2][reg],
                     hi ? acc[7][reg] : acc[3][reg]};
    *(LAS f32x4*)(base + reg * 512) = v;
  }
}
__device__ __forceinline__ v2u reduce_pair(const LAS unsigned char* src, int row, int lane) {
  float s0 = 0.f, s1 = 0.f;
#pragma unroll
  for (int ww = 0; ww < 8; ++ww) {
    const v2u t2 = *(const LAS v2u*)(src + ww * 16384 + row * 512 + lane * 8);
    s0 += __uint_as_float(t2[0]); s1 += __uint_as_float(t2[1]);
  }
  return v2u{__float_as_uint(s0), __float_as_uint(s1)};
}

__device__ __forceinline__ void lds_signal(LAS unsigned char* L, int f, int lane) {
  asm volatile("s_waitcnt lgkmcnt(0)" ::: "memory");
  if (lane == 0) __hip_atomic_fetch_add((LAS unsigned*)(L + L_FLG) + f, 1u, __ATOMIC_RELAXED, __HIP_MEMORY_SCOPE_WORKGROUP);
}
__device__ __forceinline__ void lds_await(LAS unsigned char* L, int f, unsigned n) {
  while (*((volatile LAS unsigned*)(L + L_FLG) + f) < n) __builtin_amdgcn_s_sleep(1);
  asm volatile("" ::: "memory");
}

struct Tags { unsigned a, b; };
__device__ __forceinline__ bool tags_ok(const v4u& g, const Tags& t) { return (g[1] == t.a) & (g[3] == t.b); }
__device__ __forceinline__ v4u granule(unsigned v0, unsigned v1, const Tags& t) { return v4u{v0, t.a, v1, t.b}; }
template <int N, class F> __device__ __forceinline__ void settle_f(v4u (&a)[N], F&& load, const Tags& t, int gap) {
  unsigned spins = 0;
  for (;;) {
    bool ok = true;
#pragma unroll
    for (int i = 0; i < N; ++i) ok &= tags_ok(a[i], t);
    if (__all(ok) || ++spins > SPIN_CAP) break;
    for (int i = 0; i < gap; ++i) __builtin_amdgcn_s_sleep(2);
    asm volatile("" ::: "memory");
    load(a);
    SB();
  }
}
__device__ __forceinline__ f32x4 sum_bf4(const v4u& a, const v4u& b) {
  return f32x4{bf_lo(a[0]) + bf_lo(b[0]), bf_hi(a[0]) + bf_hi(b[0]), bf_lo(a[2]) + bf_lo(b[2]), bf_hi(a[2]) + bf_hi(b[2])};
}
__device__ __forceinline__ int hf_off(int b, int G) { return WS_HF + (b * 896 + (G & ~31) + ((G & 31) >> 1) + 16 * (G & 1)) * 16; }

__device__ __forceinline__ float gelu_tanh(float g) {
  const float z2 = 1.5957691216057308f * (g + 0.044715f * g * g * g);
  return g / (1.0f + __expf(-z2));
}

__global__ void __launch_bounds__(NTHR, 3) fused_decode_kernel(Params p) {
  __shared__ __attribute__((aligned(16))) unsigned char smem[LDS_BYTES];
  LAS unsigned char* L = (LAS unsigned char*)smem;
  const int tid = threadIdx.x, lane = tid & 63, w = __builtin_amdgcn_readfirstlane(tid >> 6);
  const int g = lane >> 4, r = lane & 15;
  const int c = blockIdx.x;
  const int u = (c & 7) * 32 + (c >> 3);
  const int uj = u >> 3, ut = u & 7;
  const int xc = c & 7, ic = c >> 3;
  const bool has_gu = ic < 28;
  asm volatile("" :: "s"(p.x), "s"(p.k), "s"(p.v), "s"(p.wq), "s"(p.wo), "s"(p.wg), "s"(p.wu), "s"(p.wd), "s"(p.out), "s"(p.ws));
  SB();

  const int ca = (1 - (int)(((uintptr_t)p.wq >> 8) & 3)) & 3;
  const int rg = (1 - (int)(((uintptr_t)(xc < 4 ? p.wg : p.wu) >> 8) & 3) - (ca + 1)) & 3;
  const int ro = (1 - (int)(((uintptr_t)p.wo >> 8) & 3) - (ca + 2)) & 3;
  const int ig = (ic & ~3) | ((ic + rg) & 3);
  const int ot = (ut & 4) | ((ut + ro) & 3);

  const rsrc_t rws = mk_rsrc(p.ws, WS_END);
  const rsrc_t rx = mk_rsrc(p.x, 8 * DM * 2);
  const rsrc_t rk = mk_rsrc(p.k, 1 << 26);
  const rsrc_t rv = mk_rsrc(p.v, 1 << 26);
  const rsrc_t rwq = mk_rsrc(p.wq, DM * 4096 * 2);
  const rsrc_t rwo = mk_rsrc(p.wo, 4096 * DM * 2);
  const rsrc_t rwd = mk_rsrc(p.wd, w < 7 ? DFF * DM * 2 : 0);
  const int kq = xc & 3;
  const rsrc_t rgu = mk_rsrc(xc < 4 ? p.wg : p.wu, has_gu ? DM * DFF * 2 : 0);

#define LOAD_EPOCH() (*(const __attribute__((address_space(4))) v2u*)((const unsigned char*)p.ws + WS_EPOCH))
  Tags tg;
  if (w >= 8) {
    __builtin_amdgcn_s_setprio(3);
    const int sw = w - 8, lo16 = lane * 16;
    if (sw == 3 && lane < 8) *((LAS unsigned*)(L + L_FLG) + lane) = 0u;
    BARRIER();
    const v2u ep = LOAD_EPOCH();
    lds_await(L, F_XRAW, 8);
    v4u xr[2][2];
#pragma unroll
    for (int j = 0; j < 2; ++j)
#pragma unroll
      for (int h = 0; h < 2; ++h) xr[j][h] = *(const LAS v4u*)(L + L_X + (2 * sw + j) * XPITCH + h * 1024 + lo16);
#pragma unroll
    for (int j = 0; j < 2; ++j) {
      float xv[16];
#pragma unroll
      for (int h = 0; h < 2; ++h)
#pragma unroll
        for (int k = 0; k < 4; ++k) { xv[h * 8 + 2 * k] = bf_lo(xr[j][h][k]); xv[h * 8 + 2 * k + 1] = bf_hi(xr[j][h][k]); }
      float s = 0.f;
#pragma unroll
      for (int k = 0; k < 16; ++k) s += xv[k];
      const float mean = wave_sum(s) * (1.0f / DM);
      float q2 = 0.f;
#pragma unroll
      for (int k = 0; k < 16; ++k) { const float d = xv[k] - mean; q2 += d * d; }
      const float var = wave_sum(q2) * (1.0f / DM);
      const float mb = bfr(mean);
      const float rstd = 1.0f / bfr(sqrtf(bfr(bfr(var) + 1e-5f)));
#pragma unroll
      for (int h = 0; h < 2; ++h) {
        v4u o;
#pragma unroll
        for (int k = 0; k < 4; ++k) {
          const unsigned d2 = pack2(xv[h * 8 + 2 * k] - mb, xv[h * 8 + 2 * k + 1] - mb);
          o[k] = pack2(bf_lo(d2) * rstd, bf_hi(d2) * rstd);
        }
        *(LAS v4u*)(L + L_X + (2 * sw + j) * XPITCH + h * 1024 + lo16) = o;
      }
    }
    lds_signal(L, F_XN, lane);
    unsigned t0, t1;
    {
      const int q4 = (lane & 3) * 65536;
      if (sw == 0) {
        t0 = __builtin_amdgcn_raw_buffer_load_b32(rk, q4, u * 262144, 0);
        t1 = __builtin_amdgcn_raw_buffer_load_b32(rv, q4, u * 262144, 0);
      } else if (sw == 1) {
        t0 = __builtin_amdgcn_raw_buffer_load_b32(rwo, q4, 128 * uj * 2048, 0);
        t1 = __builtin_amdgcn_raw_buffer_load_b32(mk_rsrc(p.wd, DFF * DM * 2), q4, 112 * uj * 2048, 0);
      } else if (sw == 2) {
        t0 = __builtin_amdgcn_raw_buffer_load_b32(rws, (lane & 15) * 524288, 0, 0);
        t1 = __builtin_amdgcn_raw_buffer_load_b32(rws, WS_FP + (ot * 32 + uj) * 8192, 0, 0);
      } else {
        t0 = __builtin_amdgcn_raw_buffer_load_b32(mk_rsrc(p.out, 8 * DM * 2), (uj >> 2) * 2048 + ot * 256, 0, 0);
        t1 = __builtin_amdgcn_raw_buffer_load_b32(rws, WS_FP + ot * 262144 + q4, 0, 0);
      }
    }
    SB();
    tg.a = ep[0] + 1u;
    tg.b = (ep[0] + 1u) * 0x9E3779B1u + ep[1] + 0x7F4A7C15u;
    if (sw == 0 || sw == 3) {
      lds_await(L, F_RED1, 8);
#pragma unroll
      for (int j = 0; j < 4; ++j) {
        const int row = (sw == 3 ? 4 : 0) + j;
        const v2u s2 = reduce_pair(L + L_K + 3 * 4096, row, lane);
        st_sc1(rws, WS_QP + ((ic * 8 + row) * 8 + xc) * 1024 + lo16, granule(s2[0], s2[1], tg));
      }
      lds_signal(L, F_FREE1, lane);
    } else {
      lds_await(L, F_RED2, 8);
#pragma unroll
      for (int j = 0; j < 4; ++j) {
        const int row = (sw == 2 ? 4 : 0) + j;
        const v2u s2 = reduce_pair(L + L_K + 2 * 4096, row, lane);
        if (has_gu)
          st_sc1_b64(rws, WS_GP + ((ig * 8 + row) * 8 + xc) * 512 + lane * 8,
                     v2u{pack2(__uint_as_float(s2[0]), __uint_as_float(s2[1])), (lane & 1) ? tg.b : tg.a});
      }
      lds_signal(L, F_FREE2, lane);
    }
    asm volatile("" :: "v"(t0), "v"(t1));
    if (sw == 0) {
      v4u rq[8];
      auto ld = [&](v4u (&d)[8]) {
#pragma unroll
        for (int i = 0; i < 8; ++i) d[i] = ld_sc1(rws, WS_QP + u * 8192 + i * 1024 + lo16);
      };
      ld(rq);
      settle_f<8>(rq, ld, tg, 1);
      float s0 = 0.f, s1 = 0.f;
#pragma unroll
      for (int i = 0; i < 8; ++i) { s0 += __uint_as_float(rq[i][0]); s1 += __uint_as_float(rq[i][2]); }
      st_sc1_b64(rws, WS_QF + (ut * 32 + uj) * 512 + lane * 8, v2u{pack2(s0, s1), (lane & 1) ? tg.b : tg.a});
    } else if (sw == 1) {
      if (has_gu) {
        v4u rq[4];
        auto ld = [&](v4u (&d)[4]) {
#pragma unroll
          for (int i = 0; i < 4; ++i) d[i] = ld_sc1(rws, WS_GP + (ic * 8 + xc) * 4096 + i * 1024 + lo16);
        };
        ld(rq);
        settle_f<4>(rq, ld, tg, 1);
        const f32x4 g4 = sum_bf4(rq[0], rq[1]), u4 = sum_bf4(rq[2], rq[3]);
        float hv[4];
#pragma unroll
        for (int e = 0; e < 4; ++e) hv[e] = bfr(gelu_tanh(bfr(half_sum(g4[e])))) * bfr(half_sum(u4[e]));
        if (lane < 32) st_sc1(rws, WS_HF + (xc * 896 + 32 * ic) * 16 + lo16, granule(pack2(hv[0], hv[1]), pack2(hv[2], hv[3]), tg));
      }
    } else if (sw == 2) {
      v4u gq[4];
      const int qoff = WS_QF + ((u >> 5) * 32 + 8 * ((u >> 3) & 3)) * 512 + lo16;
      auto ld = [&](v4u (&d)[4]) {
#pragma unroll
        for (int i = 0; i < 4; ++i) d[i] = ld_sc1(rws, qoff + i * 1024);
      };
      for (int i = 0; i < G1PRE; ++i) __builtin_amdgcn_s_sleep(16);
      ld(gq);
      settle_f<4>(gq, ld, tg, G1GAP);
#pragma unroll
      for (int i = 0; i < 4; ++i)
        *(LAS v2u*)(L + L_Q + (2 * i + (lane >> 5)) * QPITCH + (2 * (lane & 15) + ((lane >> 4) & 1)) * 8) = v2u{gq[i][0], gq[i][2]};
    }
    BARRIER();
    if (sw == 1) {
      v4u gh[4];
      int off[4];
#pragma unroll
      for (int i = 0; i < 4; ++i) {
        const int idx = min(i * 64 + lane, 223), hb = idx / 28, q = idx - hb * 28;
        off[i] = hf_off(hb, 28 * uj + q);
      }
      auto ld = [&](v4u (&d)[4]) {
#pragma unroll
        for (int i = 0; i < 4; ++i) d[i] = ld_sc1(rws, off[i]);
      };
      ld(gh);
      settle_f<4>(gh, ld, tg, 4);
#pragma unroll
      for (int i = 0; i < 4; ++i) {
        const int idx = i * 64 + lane, hb = idx / 28, q = idx - hb * 28;
        if (idx < 224) *(LAS v2u*)(L + L_A2 + hb * APITCH + 256 + q * 8) = v2u{gh[i][0], gh[i][2]};
      }
      if (lane < 32) *(LAS v2u*)(L + L_A2 + (lane >> 2) * APITCH + 480 + (lane & 3) * 8) = v2u{0u, 0u};
    }
    BARRIER();
    BARRIER();
    BARRIER();
    BARRIER();
    return;
  }

  v4u xr[2];
  xr[0] = __builtin_amdgcn_raw_buffer_load_b128(rx, w * 2048 + lane * 16, 0, 0);
  xr[1] = __builtin_amdgcn_raw_buffer_load_b128(rx, w * 2048 + 1024 + lane * 16, 0, 0);
  v4u wqr[4];
  {
    const int voff = (4 * g) * 8192 + r * 16, soff = (128 * xc + 16 * w) * 8192 + ic * 256;
#pragma unroll
    for (int i = 0; i < 4; ++i) wqr[i] = ld_nt(rwq, voff, soff + i * 8192);
  }
  v4u gur[8];
  const int gu_voff = (8 * g) * 7168 + r * 16, gu_soff = (256 * kq + 32 * w) * 7168 + ig * 256;
#define LD_GU(lo, hi) _Pragma("unroll") for (int i = lo; i < hi; ++i) gur[i] = ld_nt(rgu, gu_voff, gu_soff + i * 7168)
  LD_GU(0, 2);
  SB();
  BARRIER();
  const v2u ep = LOAD_EPOCH();
  const int frow = 2 * (uj & 3) + ((tid >> 4) & 1), fcol = 128 * ot + 16 * (uj >> 2) + (tid & 15);
  const int kvbase = u * 262144 + w * 4096;
  const int lo16 = lane * 16;
  v4u kr[8][4];
  LAS unsigned char* Kw = L + L_K + w * 16384;
#define LD_K(t) _Pragma("unroll") for (int i = 0; i < 4; ++i) kr[t][i] = ld_nt(rk, lo16 + i * 1024, kvbase + (t) * 32768)
#define LD_V(buf, half, piece) _Pragma("unroll") for (int i = 0; i < 4; ++i) vr[buf][4 * (half) + i] = ld_nt(rv, lo16 + i * 1024, kvbase + (piece) * 32768)
  SB();
  PACE(6);
  LD_GU(2, 4);
  SB();
  *(LAS v4u*)(L + L_X + w * XPITCH + lane * 16) = xr[0];
  *(LAS v4u*)(L + L_X + w * XPITCH + 1024 + lane * 16) = xr[1];
  lds_signal(L, F_XRAW, lane);
  SB();
  PACE(4);
  LD_GU(4, 8);
  SB();
  PACE(4);
  LD_K(0);
  SB();
  lds_await(L, F_XN, 4);
  {
    const v2u a = *(const LAS v2u*)(L + L_X + (r & 7) * XPITCH + (128 * xc + 16 * w + 4 * g) * 2);
    f32x4 acc[8];
    zero8(acc);
    tile16(wqr, a, acc);
    store_tile(Kw + 3 * 4096, acc, g, r);
    lds_signal(L, F_RED1, lane);
  }
  SB();
  PACE(4);
  LD_K(1);
  SB();
  {
    const v4u a = *(const LAS v4u*)(L + L_X + (r & 7) * XPITCH + (256 * kq + 32 * w + 8 * g) * 2);
    f32x4 acc[8];
    zero8(acc);
    tile32(gur, a, acc);
    store_tile(Kw + 2 * 4096, acc, g, r);
    lds_signal(L, F_RED2, lane);
  }
  SB();

  auto park = [&](int slot, const v4u (&t)[4]) {
#pragma unroll
    for (int i = 0; i < 4; ++i) *(LAS v4u*)(Kw + slot * 4096 + (4 * i + g) * 256 + ((r ^ (4 * g + i)) * 16)) = t[i];
  };
  PACE(4);
  LD_K(2);
  SB();
  park(0, kr[0]);
  SB();
  PACE(4);
  LD_K(3);
  SB();
  park(1, kr[1]);
  SB();
  PACE(4);
  LD_K(4);
  SB();
  lds_await(L, F_FREE2, 2);
  park(2, kr[2]);
  SB();
  PACE(4);
  LD_K(5);
  SB();
  lds_await(L, F_FREE1, 2);
  park(3, kr[3]);
  SB();
  PACE(4);
  LD_K(6);
  SB();
  PACE(4);
  LD_K(7);
  SB();
  v4u vr[2][8];
  PACE(4);
  LD_V(0, 0, 0);
  SB();
  PACE(4);
  LD_V(0, 1, 1);
  SB();
  BARRIER();
  v4u qf[4];
#pragma unroll
  for (int s = 0; s < 4; ++s) qf[s] = *(const LAS v4u*)(L + L_Q + (r & 7) * QPITCH + s * 64 + g * 16);

  f32x4 S[8];
  const int krow = 4 * (r & 3) + (r >> 2);
  auto qk = [&](int slot) -> f32x4 {
    f32x4 acc = {0.f, 0.f, 0.f, 0.f};
#pragma unroll
    for (int s = 0; s < 4; ++s) {
      const v4u a = *(const LAS v4u*)(Kw + slot * 4096 + krow * 256 + (((4 * s + g) ^ r) * 16));
      acc = mfma32(a, qf[s], acc);
    }
    return acc;
  };
#pragma unroll
  for (int t = 0; t < 4; ++t) S[t] = qk(t);
  SB();
#pragma unroll
  for (int t = 0; t < 4; ++t) park(t, kr[4 + t]);
  SB();
  PACE(4);
  LD_V(1, 0, 2);
  SB();
#pragma unroll
  for (int t = 0; t < 4; ++t) S[4 + t] = qk(t);
  SB();
  PACE(4);
  LD_V(1, 1, 3);
  SB();

  constexpr float SCALE = 0.08838834764831845f, LOG2E = 1.4426950408889634f;
#pragma unroll
  for (int t = 0; t < 8; ++t)
#pragma unroll
    for (int e = 0; e < 4; e += 2) {
      const unsigned a2 = pack2(S[t][e], S[t][e + 1]);
      const unsigned b2 = pack2(bf_lo(a2) * SCALE, bf_hi(a2) * SCALE);
      S[t][e] = bf_lo(b2); S[t][e + 1] = bf_hi(b2);
    }
  float mx = -INFINITY;
#pragma unroll
  for (int t = 0; t < 8; ++t)
#pragma unroll
    for (int e = 0; e < 4; ++e) mx = fmaxf(mx, S[t][e]);
  mx = xrow_max(mx) * LOG2E;
  float ls = 0.f;
  v4u P[4];
#pragma unroll
  for (int T = 0; T < 4; ++T) {
    float pe[8];
#pragma unroll
    for (int e = 0; e < 8; ++e) { pe[e] = __builtin_amdgcn_exp2f(fmaf(S[2 * T + (e >> 2)][e & 3], LOG2E, -mx)); ls += pe[e]; }
    P[T] = v4u{pack2(pe[0], pe[1]), pack2(pe[2], pe[3]), pack2(pe[4], pe[5]), pack2(pe[6], pe[7])};
  }
  ls = xrow_sum(ls);
  SB();

  f32x4 O[8];
  zero8(O);
  tile32(vr[0], P[0], O);
  SB();
  PACE(4);
  LD_V(0, 0, 4);
  SB();
  PACE(4);
  LD_V(0, 1, 5);
  SB();
  tile32(vr[1], P[1], O);
  SB();
  PACE(4);
  LD_V(1, 0, 6);
  SB();
  tile32(vr[0], P[2], O);
  SB();
  PACE(4);
  LD_V(1, 1, 7);
  SB();
  v4u w2[8];
  const int w2_voff = (4 * g) * 2048 + r * 16;
  PACE(4);
  {
    const int so = (128 * uj + 16 * w) * 2048 + ot * 256;
#pragma unroll
    for (int i = 0; i < 4; ++i) w2[i] = ld_nt(rwo, w2_voff, so + i * 2048);
  }
  SB();
  PACE(4);
  {
    const int sd = (112 * uj + 16 * w) * 2048 + ot * 256;
#pragma unroll
    for (int i = 0; i < 4; ++i) w2[4 + i] = ld_nt(rwd, w2_voff, sd + i * 2048);
  }
  const unsigned xres = __builtin_amdgcn_raw_buffer_load_b32(rx, (frow * DM + (fcol & ~1)) * 2, 0, 0);
  SB();
  tile32(vr[1], P[3], O);
  tg.a = ep[0] + 1u;
  tg.b = (ep[0] + 1u) * 0x9E3779B1u + ep[1] + 0x7F4A7C15u;

  const int ab = u >> 5, akv = (u >> 3) & 3, as = u & 7;
  store_tile(Kw, O, g, r);
  if (lane < 8) *(LAS v2u*)(L + L_MM + (w * 8 + lane) * 8) = v2u{__float_as_uint(mx), __float_as_uint(ls)};
  BARRIER();
  if (tid < 256) {
    const int h = tid >> 5, pp = tid & 31, d4 = 2 * (pp & 15) + (pp >> 4);
    float mw[8], lw[8];
    float M = -INFINITY;
#pragma unroll
    for (int ww = 0; ww < 8; ++ww) {
      const v2u t2 = *(const LAS v2u*)(L + L_MM + (ww * 8 + h) * 8);
      mw[ww] = __uint_as_float(t2[0]); lw[ww] = __uint_as_float(t2[1]);
      M = fmaxf(M, mw[ww]);
    }
    f32x4 o = {0.f, 0.f, 0.f, 0.f};
    float Ls = 0.f;
#pragma unroll
    for (int ww = 0; ww < 8; ++ww) {
      const float f = __builtin_amdgcn_exp2f(mw[ww] - M);
      Ls = fmaf(f, lw[ww], Ls);
      const f32x4 t4 = *(const LAS f32x4*)(L + L_K + ww * 16384 + h * 512 + (d4 & 1) * 256 + (d4 >> 1) * 16);
      o += f * t4;
    }
    const int slot = ((8 * akv + h) * 8 + ab) * 8 + as;
    st_sc1(rws, WS_OP + slot * 512 + pp * 16, granule(pack2(o[0], o[1]), pack2(o[2], o[3]), tg));
    if (pp < 8) st_sc1(rws, WS_ML + slot * 128 + pp * 16, granule(__float_as_uint(M), __float_as_uint(Ls), tg));
  }
  SB();
  v4u bw[8];
  {
    const int obase = WS_OP + (uj * 8 + w) * 4096 + lo16, mloff = WS_ML + ((uj * 8 + w) * 8 + (lane & 7)) * 128;
    v4u go[5];
    auto load5 = [&](v4u (&d)[5]) {
      d[4] = ld_sc1(rws, mloff);
#pragma unroll
      for (int i = 0; i < 4; ++i) d[i] = ld_sc1(rws, obase + i * 1024);
    };
    for (int i = 0; i < H3PRE; ++i) __builtin_amdgcn_s_sleep(4);
    asm volatile("" ::: "memory");
    load5(go);
    SB();
#pragma unroll
    for (int i = 0; i < 8; ++i)
      *(LAS v4u*)(Kw + 8192 + (4 * g + (i & 3) + 16 * (i >> 2)) * 256 + ((r ^ (2 * ((i & 3) | ((g >> 1) << 2)))) * 16)) = w2[i];
    BARRIER();
    {
      const int q = (lane >> 2) & 3, pq = lane & 3;
      const int lane_off = (g >> 1) * 16384 + 8192 + (8 * (g & 1) + q) * 256 + (((2 * w + (pq >> 1)) ^ (2 * (q | ((g & 1) << 2)))) * 16) + (pq & 1) * 8;
#pragma unroll
      for (int s = 0; s < 8; ++s) {
        const LAS unsigned char* base = L + L_K + (2 * (s & 3)) * 16384 + (s >> 2) * 4096 + lane_off;
        const s16x4 lo = __builtin_amdgcn_ds_read_tr16_b64_v4i16((LAS s16x4*)base);
        const s16x4 hi = __builtin_amdgcn_ds_read_tr16_b64_v4i16((LAS s16x4*)(base + 4 * 256));
        const v2u l2 = __builtin_bit_cast(v2u, lo), h2 = __builtin_bit_cast(v2u, hi);
        bw[s] = v4u{l2[0], l2[1], h2[0], h2[1]};
      }
#pragma unroll
      for (int s = 0; s < 8; ++s) asm volatile("" : "+v"(bw[s]));
    }
    SB();
    settle_f<5>(go, load5, tg, 0);
    const float ms = __uint_as_float(go[4][0]), lsum = __uint_as_float(go[4][2]);
    float M = ms;
    M = fmaxf(M, dppf<XOR1>(M)); M = fmaxf(M, dppf<XOR2>(M)); M = fmaxf(M, dppf<HMIR>(M));
    const float f = __builtin_amdgcn_exp2f(ms - M);
    float Ls = f * lsum;
    Ls += dppf<XOR1>(Ls); Ls += dppf<XOR2>(Ls); Ls += dppf<HMIR>(Ls);
    const float fn = f / Ls;
    f32x4 o = {0.f, 0.f, 0.f, 0.f};
#pragma unroll
    for (int i = 0; i < 4; ++i) {
      const float we = __builtin_bit_cast(float, __builtin_amdgcn_readlane(__builtin_bit_cast(int, fn), 2 * i));
      const float wo_ = __builtin_bit_cast(float, __builtin_amdgcn_readlane(__builtin_bit_cast(int, fn), 2 * i + 1));
      const float wt = lane < 32 ? we : wo_;
      o[0] = fmaf(wt, bf_lo(go[i][0]), o[0]); o[1] = fmaf(wt, bf_hi(go[i][0]), o[1]);
      o[2] = fmaf(wt, bf_lo(go[i][2]), o[2]); o[3] = fmaf(wt, bf_hi(go[i][2]), o[3]);
    }
#pragma unroll
    for (int e = 0; e < 4; ++e) o[e] = half_sum(o[e]);
    const int d4 = 2 * (lane & 15) + ((lane >> 4) & 1);
    if (lane < 32) *(LAS v2u*)(L + L_A2 + w * APITCH + d4 * 8) = v2u{pack2(o[0], o[1]), pack2(o[2], o[3])};
  }
  BARRIER();
  {
    f32x4 acc = {0.f, 0.f, 0.f, 0.f}, acc2 = {0.f, 0.f, 0.f, 0.f};
#pragma unroll
    for (int s = 0; s < 4; ++s) {
      const v4u a0 = *(const LAS v4u*)(L + L_A2 + (r & 7) * APITCH + (32 * s + 8 * g) * 2);
      const v4u a1 = *(const LAS v4u*)(L + L_A2 + (r & 7) * APITCH + (32 * (s + 4) + 8 * g) * 2);
      acc = mfma32(a0, bw[s], acc);
      acc2 = mfma32(a1, bw[s + 4], acc2);
    }
    acc += acc2;
    const float v0 = g >= 2 ? acc[2] : acc[0], v1 = g >= 2 ? acc[3] : acc[1];
    st_sc1(rws, WS_FP + ((ot * 32 + 4 * w + 2 * (g & 1) + (g >> 1)) * 32 + uj) * 256 + r * 16,
           granule(__float_as_uint(v0), __float_as_uint(v1), tg));
  }
  SB();
  {
    const int foff = WS_FP + (ot * 32 + uj) * 8192 + w * 1024 + lo16;
    for (int i = 0; i < H4PRE; ++i) __builtin_amdgcn_s_sleep(4);
    asm volatile("" ::: "memory");
    v4u gf = ld_sc1(rws, foff);
    unsigned sp = 0;
    while (!__all(tags_ok(gf, tg)) && ++sp <= SPIN_CAP) { asm volatile("" ::: "memory"); gf = ld_sc1(rws, foff); SB(); }
    const float s0 = xrow_sum(__uint_as_float(gf[0])), s1 = xrow_sum(__uint_as_float(gf[2]));
    if (lane < 16) *(LAS v2u*)(L + L_MM + (w * 16 + lane) * 8) = v2u{__float_as_uint(s0), __float_as_uint(s1)};
    BARRIER();
    if (tid < 32) {
      float t = (fcol & 1) ? bf_hi(xres) : bf_lo(xres);
#pragma unroll
      for (int ww = 0; ww < 8; ++ww) t += *(const LAS float*)(L + L_MM + (ww * 16 + (tid & 15)) * 8 + ((tid >> 4) & 1) * 4);
      ((unsigned short*)p.out)[frow * DM + fcol] = (unsigned short)(pack2(t, 0.f) & 0xffffu);
    }
    if (u == 0 && tid == 0) {
      const unsigned long long e = (((unsigned long long)ep[1] << 32) | ep[0]) + 1ull;
      __builtin_amdgcn_raw_buffer_store_b64(v2u{(unsigned)e, (unsigned)(e >> 32)}, rws, WS_EPOCH, 0, AUX_SC1);
    }
  }
}

}

extern "C" void kernel_launch(void* const* d_in, const int* in_sizes, int n_in, void* d_out, int out_size, void* d_ws,
                              size_t ws_size, hipStream_t stream) {
  (void)in_sizes; (void)n_in; (void)out_size; (void)ws_size;
  Params p;
  p.x = d_in[0]; p.k = d_in[1]; p.v = d_in[2]; p.wq = d_in[3]; p.wo = d_in[4];
  p.wg = d_in[6]; p.wu = d_in[7]; p.wd = d_in[8];
  p.out = d_out; p.ws = d_ws;
  hipLaunchKernelGGL(fused_decode_kernel, dim3(NWG), dim3(NTHR), 0, stream, p);
}
```
